```python
import torch

def _softmax(x, axis=-1):
    return torch.softmax(x, dim=axis)
def _gelu(x):
    return torch.nn.functional.gelu(x, approximate="tanh")
def _silu(x):
    return torch.nn.functional.silu(x)
def _sigmoid(x):
    return torch.sigmoid(x)
def _layernorm(x, axis=-1, eps=1e-5):
    m = x.mean(dim=axis, keepdim=True)
    v = x.var(dim=axis, keepdim=True, unbiased=False)
    return (x - m) / torch.sqrt(v + eps)
def _scatter_add(tgt, idx, src, axis=0):
    return tgt.clone().index_add_(axis, idx, src)
def _segment_sum(src, seg, n):
    out = torch.zeros((n,) + src.shape[1:], dtype=src.dtype)
    return out.index_add_(0, seg, src)
def _pad(x, pw):
    flat = tuple(v for pair in reversed(pw) for v in pair)
    return torch.nn.functional.pad(x, flat)

kernel_name = 'concat_heads__gqa_block_cached__gqa_block_cached__near_ridge'

def setup_inputs():

    _w = (lambda a: (lambda t: t.to(torch.bfloat16) if t.is_floating_point() else t)(torch.as_tensor(a)))
    def _A_setup():
        B, Sq, D = 16, 144, 512
        Hq, Hkv, Sk, Dh = 32, 32, 512, 128
        assert Hq % Hkv == 0, (Hq, Hkv)
        return {
            "x": _w((lambda *s: torch.randn(*s, dtype=torch.bfloat16) * 0.8)(B, Sq, D)),
            "k_cache": _w((lambda *s: torch.randn(*s, dtype=torch.bfloat16) * 0.8)(B, Hkv, Sk, Dh)),
            "v_cache": _w((lambda *s: torch.randn(*s, dtype=torch.bfloat16) * 0.05)(B, Hkv, Sk, Dh)),
            "wq": _w((lambda *s: torch.randn(*s, dtype=torch.bfloat16) * 0.8)(D, Hq * Dh) * (D ** -0.5)),
            "wo": _w((lambda *s: torch.randn(*s, dtype=torch.bfloat16) * 0.05)(Hq * Dh, D) * ((Hq * Dh) ** -0.5)),
        }
    _A_in = _A_setup()

    def _B_setup():
        B, Sq, D = 16, 144, 512
        Hq, Hkv, Sk, Dh = 32, 32, 512, 128
        assert Hq % Hkv == 0, (Hq, Hkv)
        return {
            "x": _w((lambda *s: torch.randn(*s, dtype=torch.bfloat16) * 0.8)(B, Sq, D)),
            "k_cache": _w((lambda *s: torch.randn(*s, dtype=torch.bfloat16) * 0.8)(B, Hkv, Sk, Dh)),
            "v_cache": _w((lambda *s: torch.randn(*s, dtype=torch.bfloat16) * 0.05)(B, Hkv, Sk, Dh)),
            "wq": _w((lambda *s: torch.randn(*s, dtype=torch.bfloat16) * 0.8)(D, Hq * Dh) * (D ** -0.5)),
            "wo": _w((lambda *s: torch.randn(*s, dtype=torch.bfloat16) * 0.05)(Hq * Dh, D) * ((Hq * Dh) ** -0.5)),
        }
    _B_in = _B_setup()

    return {
        "A_x": _A_in["x"],
        "A_k_cache": _A_in["k_cache"],
        "A_v_cache": _A_in["v_cache"],
        "A_wq": _A_in["wq"],
        "A_wo": _A_in["wo"],
        "B_x": _B_in["x"],
        "B_k_cache": _B_in["k_cache"],
        "B_v_cache": _B_in["v_cache"],
        "B_wq": _B_in["wq"],
        "B_wo": _B_in["wo"],
    }

def reference(A_x, A_k_cache, A_v_cache, A_wq, A_wo, B_x, B_k_cache, B_v_cache, B_wq, B_wo):
    def _A_ref(x, k_cache, v_cache, wq, wo):
        B, Sq, D = x.shape
        Hq, Dh = 32, 128
        Hkv = k_cache.shape[1]
        rep = Hq // Hkv
        q = (lambda x, *d: x.permute(*d))((x @ wq).reshape(B, Sq, Hq, Dh), 0, 2, 1, 3)
        k = (lambda x, n, axis: torch.repeat_interleave(x, n, dim=axis))(k_cache, rep, axis=1)
        v = (lambda x, n, axis: torch.repeat_interleave(x, n, dim=axis))(v_cache, rep, axis=1)
        scores = torch.einsum("bhqd,bhkd->bhqk", q, k) * (Dh ** -0.5)
        p = _softmax(scores, axis=-1)
        o = torch.einsum("bhqk,bhkd->bhqd", p, v)
        return (lambda x, *d: x.permute(*d))(o, 0, 2, 1, 3).reshape(B, Sq, Hq * Dh) @ wo

    def _B_ref(x, k_cache, v_cache, wq, wo):
        B, Sq, D = x.shape
        Hq, Dh = 32, 128
        Hkv = k_cache.shape[1]
        rep = Hq // Hkv
        q = (lambda x, *d: x.permute(*d))((x @ wq).reshape(B, Sq, Hq, Dh), 0, 2, 1, 3)
        k = (lambda x, n, axis: torch.repeat_interleave(x, n, dim=axis))(k_cache, rep, axis=1)
        v = (lambda x, n, axis: torch.repeat_interleave(x, n, dim=axis))(v_cache, rep, axis=1)
        scores = torch.einsum("bhqd,bhkd->bhqk", q, k) * (Dh ** -0.5)
        p = _softmax(scores, axis=-1)
        o = torch.einsum("bhqk,bhkd->bhqd", p, v)
        return (lambda x, *d: x.permute(*d))(o, 0, 2, 1, 3).reshape(B, Sq, Hq * Dh) @ wo

    a = _A_ref(A_x, A_k_cache, A_v_cache, A_wq, A_wo)
    b = _B_ref(B_x, B_k_cache, B_v_cache, B_wq, B_wo)
    return torch.concatenate([a, b], axis=-1)
```

```cpp
#include <hip/hip_runtime.h>
#include <hip/hip_bf16.h>
typedef __hip_bfloat16 bf16;
constexpr int B=16, SQ=144, D=512, H=32, SK=512, DH=128;

__global__ void gemm_naive(const bf16* A, const bf16* Bm, bf16* C, int M, int N, int K, int ldc) {
  __shared__ float As[16][17], Bs[16][17];
  int tx = threadIdx.x & 15, ty = threadIdx.x >> 4;
  int row = blockIdx.y * 16 + ty, col = blockIdx.x * 16 + tx;
  float acc = 0;
  for (int k0 = 0; k0 < K; k0 += 16) {
    As[ty][tx] = __bfloat162float(A[(long)row * K + k0 + tx]);
    Bs[ty][tx] = __bfloat162float(Bm[(long)(k0 + ty) * N + col]);
    __syncthreads();
    for (int k = 0; k < 16; ++k) acc += As[ty][k] * Bs[k][tx];
    __syncthreads();
  }
  C[(long)row * ldc + col] = __float2bfloat16(acc);
}
__global__ void attn_naive(const bf16* Q, const bf16* Kc, const bf16* Vc, bf16* O) {
  __shared__ float q[DH], s[SK], red[128];
  int qi = blockIdx.x, h = blockIdx.y, b = blockIdx.z, t = threadIdx.x;
  q[t] = __bfloat162float(Q[((long)(b * SQ + qi)) * (H * DH) + h * DH + t]);
  __syncthreads();
  const bf16* Kp = Kc + ((long)(b * H + h)) * SK * DH;
  const bf16* Vp = Vc + ((long)(b * H + h)) * SK * DH;
  float mx = -1e30f;
  for (int k = t; k < SK; k += 128) {
    float a = 0;
    for (int d = 0; d < DH; ++d) a += q[d] * __bfloat162float(Kp[(long)k * DH + d]);
    a *= 0.08838834764831845f;
    s[k] = a; mx = fmaxf(mx, a);
  }
  red[t] = mx; __syncthreads();
  for (int o = 64; o > 0; o >>= 1) { if (t < o) red[t] = fmaxf(red[t], red[t + o]); __syncthreads(); }
  mx = red[0]; __syncthreads();
  float sum = 0;
  for (int k = t; k < SK; k += 128) { float e = __expf(s[k] - mx); s[k] = e; sum += e; }
  red[t] = sum; __syncthreads();
  for (int o = 64; o > 0; o >>= 1) { if (t < o) red[t] += red[t + o]; __syncthreads(); }
  sum = red[0];
  float a = 0;
  for (int k = 0; k < SK; ++k) a += s[k] * __bfloat162float(Vp[(long)k * DH + t]);
  O[((long)(b * SQ + qi)) * (H * DH) + h * DH + t] = __float2bfloat16(a / sum);
}
extern "C" void kernel_launch(void* const* d_in, const int* in_sizes, int n_in, void* d_out, int out_size, void* d_ws, size_t ws_size, hipStream_t stream) {
  bf16* Q = (bf16*)d_ws; bf16* O = Q + (size_t)B * SQ * H * DH;
  for (int br = 0; br < 2; ++br) {
    const bf16* x = (const bf16*)d_in[br * 5 + 0]; const bf16* kc = (const bf16*)d_in[br * 5 + 1]; const bf16* vc = (const bf16*)d_in[br * 5 + 2];
    const bf16* wq = (const bf16*)d_in[br * 5 + 3]; const bf16* wo = (const bf16*)d_in[br * 5 + 4];
    gemm_naive<<<dim3(H * DH / 16, B * SQ / 16), 256, 0, stream>>>(x, wq, Q, B * SQ, H * DH, D, H * DH);
    attn_naive<<<dim3(SQ, H, B), 128, 0, stream>>>(Q, kc, vc, O);
    gemm_naive<<<dim3(D / 16, B * SQ / 16), 256, 0, stream>>>(O, wo, (bf16*)d_out + br * D, B * SQ, D, H * DH, 2 * D);
  }
}
```

```cpp
#define ACLOB "a0","a1","a2","a3","a4","a5","a6","a7","a8","a9","a10","a11","a12","a13","a14","a15","a16","a17","a18","a19","a20","a21","a22","a23","a24","a25","a26","a27","a28","a29","a30","a31","a32","a33","a34","a35","a36","a37","a38","a39","a40","a41","a42","a43","a44","a45","a46","a47","a48","a49","a50","a51","a52","a53","a54","a55","a56","a57","a58","a59","a60","a61","a62","a63","a64","a65","a66","a67","a68","a69","a70","a71","a72","a73","a74","a75","a76","a77","a78","a79","a80","a81","a82","a83","a84","a85","a86","a87","a88","a89","a90","a91","a92","a93","a94","a95","a96","a97","a98","a99","a100","a101","a102","a103","a104","a105","a106","a107","a108","a109","a110","a111","a112","a113","a114","a115","a116","a117","a118","a119","a120","a121","a122","a123","a124","a125","a126","a127","a128","a129","a130","a131","a132","a133","a134","a135","a136","a137","a138","a139","a140","a141","a142","a143","a144","a145","a146","a147","a148","a149","a150","a151","a152","a153","a154","a155","a156","a157","a158","a159","a160","a161","a162","a163","a164","a165","a166","a167","a168","a169","a170","a171","a172","a173","a174","a175","a176","a177","a178","a179","a180","a181","a182","a183","a184","a185","a186","a187","a188","a189","a190","a191","a192","a193","a194","a195","a196","a197","a198","a199","a200","a201","a202","a203","a204","a205","a206","a207","a208","a209","a210","a211","a212","a213","a214","a215","a216","a217","a218","a219","a220","a221","a222","a223","a224","a225","a226","a227","a228","a229","a230","a231","a232","a233","a234","a235","a236","a237","a238","a239","a240","a241","a242","a243","a244","a245","a246","a247","a248","a249","a250","a251","a252","a253","a254","a255"
#include <hip/hip_runtime.h>
#include <hip/hip_bf16.h>
#include <utility>

typedef __bf16 bf16x8 __attribute__((ext_vector_type(8)));
typedef __bf16 bf16x4 __attribute__((ext_vector_type(4)));
typedef __bf16 bf16x2 __attribute__((ext_vector_type(2)));
typedef float f32x4 __attribute__((ext_vector_type(4)));
typedef float f32x2 __attribute__((ext_vector_type(2)));
typedef unsigned u32x4 __attribute__((ext_vector_type(4)));
#define DI __device__ __forceinline__
#define GAP() __builtin_amdgcn_sched_barrier(0)
#define PIN(x) asm volatile("" : "+v"(x))
typedef __attribute__((address_space(3))) bf16x8* lds8_t;
typedef __attribute__((address_space(3))) bf16x4* lds4_t;
#define LDS8(off) ((lds8_t)(size_t)(off))
#define LDS4(off) ((lds4_t)(size_t)(off))

constexpr unsigned SLOT = 32768, XST = 131072, XSTAGE = 9216;
constexpr unsigned FLAGS = XST + 3 * XSTAGE;
constexpr unsigned LDS_BYTES = FLAGS + 64;
constexpr unsigned PART_BYTES = 147456;
constexpr float QSCALE = 0.12751743082f;
constexpr float LBIG = 16777216.f;
constexpr float LTHR = 1024.f;
constexpr int AR = 160;
constexpr unsigned WST = 2 * SLOT, WSTAGE = 16384;


template <class F, int... I> DI void sfor_(F&& f, std::integer_sequence<int, I...>) { (f(std::integral_constant<int, I>{}), ...); }
template <int N, class F> DI void sfor(F&& f) { sfor_(f, std::make_integer_sequence<int, N>{}); }

DI unsigned lane_id() { unsigned l; asm volatile("v_mbcnt_lo_u32_b32 %0, -1, 0\n\tv_mbcnt_hi_u32_b32 %0, -1, %0" : "=v"(l)); return l; }
DI unsigned rfl(unsigned x) { return __builtin_amdgcn_readfirstlane(x); }
DI unsigned cvtpk(float lo, float hi) { f32x2 v = {lo, hi}; bf16x2 b = __builtin_convertvector(v, bf16x2); return __builtin_bit_cast(unsigned, b); }
DI bf16x8 asb(const u32x4& w) { return __builtin_bit_cast(bf16x8, w); }

template <int D> DI void mfma_acc(const bf16x8& a, const bf16x8& b) {
  asm volatile("v_mfma_f32_16x16x32_bf16 a[%c2:%c3], %0, %1, a[%c2:%c3]" :: "v"(a), "v"(b), "i"(D), "i"(D + 3) : ACLOB);
}
template <int D> DI void mfma_acc0(const bf16x8& a, const bf16x8& b) {
  asm volatile("v_mfma_f32_16x16x32_bf16 a[%c2:%c3], %0, %1, 0" :: "v"(a), "v"(b), "i"(D), "i"(D + 3) : ACLOB);
}
template <int D> DI void zero16(const bf16x8& z) {
  asm volatile("v_mfma_f32_32x32x16_bf16 a[%c1:%c2], %0, %0, 0" :: "v"(z), "i"(D), "i"(D + 15) : ACLOB);
}
DI void mfma_v(f32x4& d, const bf16x8& a, const bf16x8& b) { asm volatile("v_mfma_f32_16x16x32_bf16 %0, %1, %2, %0" : "+v"(d) : "v"(a), "v"(b) : ACLOB); }
DI void mfma_v0(f32x4& d, const bf16x8& a, const bf16x8& b) { asm volatile("v_mfma_f32_16x16x32_bf16 %0, %1, %2, 0" : "=&v"(d) : "v"(a), "v"(b) : ACLOB); }
DI void mfma_s0(f32x4& s, const bf16x8& k, const u32x4& q, const f32x4& c0) {
  asm volatile("v_mfma_f32_16x16x32_bf16 %0, %1, %2, %3" : "=&v"(s) : "v"(k), "v"(q), "v"(c0) : ACLOB);
}
DI void mfma_s(f32x4& s, const bf16x8& k, const u32x4& q) { asm volatile("v_mfma_f32_16x16x32_bf16 %0, %1, %2, %0" : "+v"(s) : "v"(k), "v"(q) : ACLOB); }
template <int R> DI float acc_rd() { float x; asm volatile("v_accvgpr_read_b32 %0, a[%c1]" : "=v"(x) : "i"(R)); return x; }
template <int R> DI void acc_wr(unsigned x) { asm volatile("v_accvgpr_write_b32 a[%c1], %0" :: "v"(x), "i"(R)); }
template <int R> DI void acc_zero() { asm volatile("v_accvgpr_write_b32 a[%c0], 0" :: "i"(R)); }
template <int N> DI void wait_bar() { asm volatile("s_waitcnt vmcnt(%c0) lgkmcnt(0)\n\ts_barrier" :: "i"(N) : "memory"); }
#define RAW_OPS(r) "+v"(r[0]), "+v"(r[1]), "+v"(r[2]), "+v"(r[3]), "+v"(r[4]), "+v"(r[5]), "+v"(r[6]), "+v"(r[7])
template <int N> DI void wait_raw(u32x4 (&r)[8]) { asm volatile("s_waitcnt vmcnt(%c8)" : RAW_OPS(r) : "i"(N) : "memory"); }

struct Ctx {
  unsigned* fw; unsigned hg;
  __amdgpu_buffer_rsrc_t srdK, srdV, srdX, srdW;
  unsigned wid, ldsb, m0w, rot;
  unsigned vk2[2], vv2[2];
  unsigned kb_l, vb_l;
};

template <int OFF> DI void set_m0(unsigned base) { asm volatile("s_add_u32 m0, %0, %c1" :: "s"(base), "i"(OFF) : "scc", "memory"); }
template <int OFF> DI void dma1(const __amdgpu_buffer_rsrc_t& srd, unsigned voff, unsigned so) {
  asm volatile("buffer_load_dwordx4 %0, %1, %2 offen offset:%c3 nt lds" :: "v"(voff), "s"(srd), "s"(so), "i"(OFF) : "memory");
}
DI unsigned kv_so(unsigned s, unsigned lim) { return s < lim ? (s + (s & 16u)) << 13 : 0x80000u; }
template <int H> DI void kv_m0(unsigned m) { set_m0<(H >> 2) * 8192 + ((H >> 1) & 1) * 4096 + (H & 1) * 1024>(m); }
template <int H, int I> DI void kv_dma(const Ctx& c, unsigned so, unsigned so2) {
  dma1<2048 * I>((H >> 2) ? c.srdV : c.srdK, (H >> 2) ? c.vv2[H & 1] : c.vk2[H & 1], ((H >> 1) & 1) ? so2 : so);
}
DI void dma_kv(const Ctx& c, unsigned s, unsigned lim) {
  const unsigned m = c.m0w + (s & 3u) * SLOT, o = kv_so(s, lim), o2 = o + 4096u;
  sfor<8>([&](auto H) { kv_m0<H>(m); asm volatile("s_nop 0"); kv_dma<H, 0>(c, o, o2); kv_dma<H, 1>(c, o, o2); });
}
DI void dma_any(const __amdgpu_buffer_rsrc_t& srd, unsigned voff, unsigned so) { asm volatile("buffer_load_dwordx4 %0, %1, %2 offen lds" :: "v"(voff), "s"(srd), "s"(so) : "memory"); }
template <int OFF> DI void dma_off(const __amdgpu_buffer_rsrc_t& srd, unsigned voff, unsigned so) { asm volatile("buffer_load_dwordx4 %0, %1, %2 offen offset:%c3 lds" :: "v"(voff), "s"(srd), "s"(so), "i"(OFF) : "memory"); }
template <int OFF> DI void dma_nt(const __amdgpu_buffer_rsrc_t& srd, unsigned voff, unsigned so) { asm volatile("buffer_load_dwordx4 %0, %1, %2 offen offset:%c3 nt lds" :: "v"(voff), "s"(srd), "s"(so), "i"(OFF) : "memory"); }
DI void set_m0s(unsigned a, unsigned b) { asm volatile("s_add_u32 m0, %0, %1" :: "s"(a), "s"(b) : "scc", "memory"); }
DI void ld_row(u32x4& r, unsigned voff, const __amdgpu_buffer_rsrc_t& srd, unsigned so) {
  asm volatile("buffer_load_dwordx4 %0, %1, %2, %3 offen" : "=v"(r) : "v"(voff), "s"(srd), "s"(so) : "memory");
}
template <int OFF> DI void ld_row_o(u32x4& r, unsigned voff, const __amdgpu_buffer_rsrc_t& srd, unsigned so) {
  asm volatile("buffer_load_dwordx4 %0, %1, %2, %3 offen offset:%c4" : "=v"(r) : "v"(voff), "s"(srd), "s"(so), "i"(OFF) : "memory");
}
template <int T, int M> DI void perm1(u32x4& w, const u32x4 (&raw)[8]) {
  w[M] = __builtin_amdgcn_perm(raw[2 * M + 1][T >> 1], raw[2 * M][T >> 1], (T & 1) ? 0x07060302u : 0x05040100u);
}

struct QL {
  unsigned vw[4], wm0;
  unsigned vx[2], xm0;
};
struct QR { unsigned xb, wb[4]; };
#define KK0(k) ((k) + 16u * ((271u - 13u * 17u - (k)) / 16u))
constexpr int QN = 13;
DI void ql_init(const Ctx& c, QL& q) {
  const unsigned ln = lane_id(), g = ln >> 4, i16 = ln & 15u, p = c.wid >> 1;
  const unsigned wl = g * 8192u + 16u * (i16 ^ g);
  sfor<4>([&](auto J) { q.vw[J] = wl + J * (32768u - 1024u); });
  q.wm0 = c.ldsb + WST + p * 8192u;
  const unsigned rx = ln >> 2, vx = rx * 1024u + 16u * ((ln & 3u) ^ ((0u - (rx >> 2)) & 3u));
  q.vx[0] = vx + p * 4u * 16384u; q.vx[1] = q.vx[0] + (16384u - 1024u);
  q.xm0 = c.ldsb + XST + p * 4096u;
}
template <int J> DI void q_m0(const QL& q, unsigned ws, unsigned xs) {
  if constexpr (J < 8) asm volatile("s_add_u32 m0, %0, %1\n\ts_add_u32 m0, m0, %c2" :: "s"(q.wm0), "s"(ws), "i"((J >> 2) * 4096) : "scc", "memory");
  else asm volatile("s_add_u32 m0, %0, %1\n\ts_add_u32 m0, m0, %c2" :: "s"(q.xm0), "s"(xs), "i"((J - 8) * 1024) : "scc", "memory");
}
template <int J, bool NT> DI void q_dma(const Ctx& c, const QL& q, unsigned sow, unsigned sox) {
  if constexpr (J < 8) { if constexpr (NT) dma_nt<1024 * (J & 3)>(c.srdW, q.vw[J & 3], sow + (J >> 2) * 131072u); else dma_off<1024 * (J & 3)>(c.srdW, q.vw[J & 3], sow + (J >> 2) * 131072u); }
  else { constexpr int k = J - 8; if constexpr (NT) dma_nt<1024 * (k & 1)>(c.srdX, q.vx[k & 1], sox + (k >> 1) * 32768u); else dma_off<1024 * (k & 1)>(c.srdX, q.vx[k & 1], sox + (k >> 1) * 32768u); }
}
DI void q_pre(const Ctx& c, unsigned hoff) {
  QL q; ql_init(c, q);
  sfor<3>([&](auto K) {
    const unsigned kk = (c.rot + K) & 15u;
    sfor<QN>([&](auto J) {
      if constexpr (J == 0 || J == 4 || J == 8 || J == 10 || J == 12) { q_m0<J>(q, K * WSTAGE, K * XSTAGE); asm volatile("s_nop 0"); }
      q_dma<J, true>(c, q, hoff + (kk << 18), kk << 6);
    });
  });
}
template <int STG, int T> DI bf16x8 wfrag(const QR& q) {
  const bf16x4 lo = __builtin_amdgcn_ds_read_tr16_b64_v4bf16(LDS4(q.wb[T >> 1] + STG * WSTAGE + 8u * (T & 1)));
  const bf16x4 hi = __builtin_amdgcn_ds_read_tr16_b64_v4bf16(LDS4(q.wb[T >> 1] + STG * WSTAGE + 8u * (T & 1) + 1024u));
  return __builtin_shufflevector(lo, hi, 0, 1, 2, 3, 4, 5, 6, 7);
}
template <int NB, int STG>
DI void qstep(const Ctx& c, const QL& ql, const QR& q, unsigned hoff, unsigned& kk, unsigned& xs, f32x4 (&acc)[NB][8], bf16x8 (&wf)[3], bf16x8 (&xf)[NB], bf16x8 (&xg)[NB]) {
  constexpr bool LD = NB == 4;
  asm volatile("s_waitcnt vmcnt(%c0) lgkmcnt(4)\n\ts_barrier" :: "i"(LD ? QN : 63) : "memory");
  unsigned xn = xs + XSTAGE; if (xn == 3 * XSTAGE) xn = 0;
  const unsigned xb = q.xb + xn;
  const unsigned k4 = kk & 15u;
  const unsigned sow = kk < 256u ? hoff + (k4 << 18) : 0x7f000000u, sox = kk < 256u ? k4 << 6 : 0x7f000000u;
  if constexpr (LD) kk += 17u;
  GAP();
  sfor<8>([&](auto T) {
    constexpr int t = T;
    sfor<NB>([&](auto B) {
      constexpr int b = B;
      mfma_v(acc[b][t], wf[(8 * STG + t) % 3], xf[b]);
      GAP();
      if constexpr (b == 1 && t < NB) xg[t] = *LDS8(xb + t * 1024u);
      if constexpr (b == 0) { if constexpr (t < 6) wf[(8 * STG + t + 2) % 3] = wfrag<STG, t + 2>(q); else wf[(8 * STG + t + 2) % 3] = wfrag<(STG + 1) & 3, t - 6>(q); }
      if constexpr (LD && b >= 2) {
        constexpr int j = 2 * t + (b - 2);
        if constexpr (j < QN) q_dma<j, false>(c, ql, sow, sox);
      }
      if constexpr (LD && b == 1) {
        constexpr int j = 2 * t;
        if constexpr (j == 0 || j == 4 || j == 8 || j == 10 || j == 12) q_m0<j>(ql, ((STG + 3) & 3) * WSTAGE, xs);
      }
      GAP();
    });
  });
  xs = xn;
}

template <int NB>
DI void qproj(const Ctx& c, unsigned hoff, unsigned ph, unsigned e0, unsigned f2, u32x4 (&Q)[NB][4]) {
  constexpr bool LD = NB == 4;
  bf16x8 xf[NB], xg[NB], wf[3];
  f32x4 acc[NB][8];
  QL ql; QR q;
  if (ph == 1u) wait_bar<LD ? 0 : 63>();
  if constexpr (LD) { if (ph < 2u) q_pre(c, hoff); }
  if constexpr (LD) ql_init(c, ql);
  {
    const unsigned ln = lane_id(), g = ln >> 4, i16 = ln & 15u, qq = i16 >> 2, pp = ln & 3u;
    sfor<4>([&](auto S) { q.wb[S] = c.ldsb + WST + (c.wid >> 1) * 8192u + (8u * g + qq) * 256u + 64u * pp + 16u * (S ^ qq); });
    q.xb = c.ldsb + XST + (LD ? 5120u : 0u) + i16 * 64u + 16u * (g ^ ((0u - (i16 >> 2)) & 3u));
  }
  sfor<NB>([&](auto B) { sfor<8>([&](auto T) { acc[B][T] = f32x4{0.f, 0.f, 0.f, 0.f}; PIN(acc[B][T]); }); });
  wait_bar<LD ? 2 * QN : 63>();
  sfor<NB>([&](auto B) { xf[B] = *LDS8(q.xb + B * 1024u); });
  wf[0] = wfrag<0, 0>(q); wf[1] = wfrag<0, 1>(q);
  GAP();
  unsigned kk = ((c.rot + 3u) & 15u) + 0u, xs = 0;
  kk = KK0(kk);
  for (unsigned it = 0; it < 4; ++it) {
    if constexpr (!LD) { if (it == 2u && e0 < 32u) { dma_kv(c, e0, 32); dma_kv(c, e0 + 1u, 32); } }
    qstep<NB, 0>(c, ql, q, hoff, kk, xs, acc, wf, xf, xg);
    qstep<NB, 1>(c, ql, q, hoff, kk, xs, acc, wf, xg, xf);
    qstep<NB, 2>(c, ql, q, hoff, kk, xs, acc, wf, xf, xg);
    qstep<NB, 3>(c, ql, q, hoff, kk, xs, acc, wf, xg, xf);
    { const bf16x8 w0 = wf[2], w1 = wf[0]; wf[0] = w0; wf[1] = w1; }
    asm volatile("s_nop 1" : "+v"(wf[0]), "+v"(wf[1]));
  }
  wait_bar<LD ? 0 : 63>();
  if constexpr (LD) { if (f2 < 32u) dma_kv(c, f2, 32); }
  asm volatile("s_nop 3" : "+v"(acc[NB - 1][7]));
  sfor<NB>([&](auto B) { sfor<4>([&](auto S) {
    const f32x4 lo = acc[B][2 * S] * QSCALE, hi = acc[B][2 * S + 1] * QSCALE;
    Q[B][S] = u32x4{cvtpk(lo[0], lo[1]), cvtpk(lo[2], lo[3]), cvtpk(hi[0], hi[1]), cvtpk(hi[2], hi[3])};
  }); });
}

DI float max3f(float a, float b, float c) { float r; asm("v_max3_f32 %0, %1, %2, %3" : "=v"(r) : "v"(a), "v"(b), "v"(c)); return r; }
DI float max2f(float a, float b) { float r; asm("v_max_f32_e32 %0, %1, %2" : "=v"(r) : "v"(a), "v"(b)); return r; }
DI float xmax16(float x) { auto a = __builtin_amdgcn_permlane16_swap(__float_as_uint(x), __float_as_uint(x), false, false); return max2f(__uint_as_float(a[0]), __uint_as_float(a[1])); }
DI float xmax32(float x) { auto a = __builtin_amdgcn_permlane32_swap(__float_as_uint(x), __float_as_uint(x), false, false); return max2f(__uint_as_float(a[0]), __uint_as_float(a[1])); }
DI float xsum4(float x) {
  auto a = __builtin_amdgcn_permlane16_swap(__float_as_uint(x), __float_as_uint(x), false, false);
  x = __uint_as_float(a[0]) + __uint_as_float(a[1]);
  auto b = __builtin_amdgcn_permlane32_swap(__float_as_uint(x), __float_as_uint(x), false, false);
  return __uint_as_float(b[0]) + __uint_as_float(b[1]);
}
template <int NB> DI void bstep() { wait_bar<NB == 4 ? 32 : 63>(); }

template <int NB> struct Att {
  f32x4 negm[NB]; float l[NB];
  bf16x8 kf[2][4], vf[8];
  f32x4 S[2];
  float p[8];
  u32x4 P;
  float tsp;
  unsigned vslot, s, lim;
};
enum { K_FIRST = 0, K_FAST = 1, K_LAST = 2 };
template <int NB, int KIND> DI void astep_gen(const Ctx& c, Att<NB>& A, const u32x4 (&Q)[NB][4]);
#define BSTEP(n) bstep<n>()
template <> DI void astep_gen<5, K_FAST>(const Ctx& c, Att<5>& A, const u32x4 (&Q)[5][4]) {
  f32x4 S0[2], S1[2], S2[2]; S0[0] = A.S[0]; S0[1] = A.S[1]; float p0[8], p1[8]; u32x4 P0, P1; bf16x4 vlo[8], vhi[8];
  mfma_s0(S1[0], A.kf[0][0], Q[1][0], A.negm[1]); GAP(); const unsigned vb0 = c.vb_l + A.vslot, vb1 = vb0 ^ 32u; GAP();
  mfma_s(S1[0], A.kf[0][1], Q[1][1]); GAP(); p0[0] = __builtin_amdgcn_exp2f(S0[0][0]); GAP();
  mfma_s(S1[0], A.kf[0][2], Q[1][2]); GAP(); p0[1] = __builtin_amdgcn_exp2f(S0[0][1]); GAP();
  mfma_s(S1[0], A.kf[0][3], Q[1][3]); GAP(); p0[2] = __builtin_amdgcn_exp2f(S0[0][2]); GAP();
  mfma_s0(S1[1], A.kf[1][0], Q[1][0], A.negm[1]); GAP(); p0[3] = __builtin_amdgcn_exp2f(S0[0][3]); GAP();
  mfma_s(S1[1], A.kf[1][1], Q[1][1]); GAP(); P0[0] = cvtpk(p0[0], p0[1]); GAP(); A.l[0] += p0[0]; PIN(A.l[0]); GAP();
  mfma_s(S1[1], A.kf[1][2], Q[1][2]); GAP(); p0[4] = __builtin_amdgcn_exp2f(S0[1][0]); GAP();
  mfma_s(S1[1], A.kf[1][3], Q[1][3]); GAP(); p0[5] = __builtin_amdgcn_exp2f(S0[1][1]); GAP();
  mfma_acc<128>(A.vf[0], asb(A.P)); GAP(); vlo[0] = __builtin_amdgcn_ds_read_tr16_b64_v4bf16(LDS4(vb0 + 0u)); GAP(); vhi[0] = __builtin_amdgcn_ds_read_tr16_b64_v4bf16(LDS4(vb0 + 4096u)); GAP();
  mfma_acc<132>(A.vf[1], asb(A.P)); GAP(); vlo[1] = __builtin_amdgcn_ds_read_tr16_b64_v4bf16(LDS4(vb1 + 0u)); GAP(); vhi[1] = __builtin_amdgcn_ds_read_tr16_b64_v4bf16(LDS4(vb1 + 4096u)); GAP();
  mfma_acc<136>(A.vf[2], asb(A.P)); GAP(); vlo[2] = __builtin_amdgcn_ds_read_tr16_b64_v4bf16(LDS4(vb0 + 512u)); GAP(); vhi[2] = __builtin_amdgcn_ds_read_tr16_b64_v4bf16(LDS4(vb0 + 4608u)); GAP();
  mfma_acc<140>(A.vf[3], asb(A.P)); GAP(); vlo[3] = __builtin_amdgcn_ds_read_tr16_b64_v4bf16(LDS4(vb1 + 512u)); GAP(); vhi[3] = __builtin_amdgcn_ds_read_tr16_b64_v4bf16(LDS4(vb1 + 4608u)); GAP();
  mfma_acc<144>(A.vf[4], asb(A.P)); GAP(); vlo[4] = __builtin_amdgcn_ds_read_tr16_b64_v4bf16(LDS4(vb0 + 1024u)); GAP(); vhi[4] = __builtin_amdgcn_ds_read_tr16_b64_v4bf16(LDS4(vb0 + 5120u)); GAP();
  mfma_acc<148>(A.vf[5], asb(A.P)); GAP(); vlo[5] = __builtin_amdgcn_ds_read_tr16_b64_v4bf16(LDS4(vb1 + 1024u)); GAP(); vhi[5] = __builtin_amdgcn_ds_read_tr16_b64_v4bf16(LDS4(vb1 + 5120u)); GAP();
  mfma_acc<152>(A.vf[6], asb(A.P)); GAP(); vlo[6] = __builtin_amdgcn_ds_read_tr16_b64_v4bf16(LDS4(vb0 + 1536u)); GAP(); vhi[6] = __builtin_amdgcn_ds_read_tr16_b64_v4bf16(LDS4(vb0 + 5632u)); GAP();
  mfma_acc<156>(A.vf[7], asb(A.P)); GAP(); vlo[7] = __builtin_amdgcn_ds_read_tr16_b64_v4bf16(LDS4(vb1 + 1536u)); GAP(); vhi[7] = __builtin_amdgcn_ds_read_tr16_b64_v4bf16(LDS4(vb1 + 5632u)); GAP();
  mfma_s0(S2[0], A.kf[0][0], Q[2][0], A.negm[2]); GAP(); P0[1] = cvtpk(p0[2], p0[3]); GAP(); A.l[0] += p0[1]; PIN(A.l[0]); GAP();
  mfma_s(S2[0], A.kf[0][1], Q[2][1]); GAP(); p0[6] = __builtin_amdgcn_exp2f(S0[1][2]); GAP();
  mfma_s(S2[0], A.kf[0][2], Q[2][2]); GAP(); p0[7] = __builtin_amdgcn_exp2f(S0[1][3]); GAP();
  mfma_s(S2[0], A.kf[0][3], Q[2][3]); GAP(); P0[2] = cvtpk(p0[4], p0[5]); GAP(); A.l[0] += p0[2]; PIN(A.l[0]); GAP();
  mfma_s0(S2[1], A.kf[1][0], Q[2][0], A.negm[2]); GAP(); P0[3] = cvtpk(p0[6], p0[7]); PIN(P0); GAP(); A.l[0] += p0[3]; PIN(A.l[0]); GAP();
  mfma_s(S2[1], A.kf[1][1], Q[2][1]); GAP(); A.l[0] += p0[4]; PIN(A.l[0]); GAP();
  mfma_s(S2[1], A.kf[1][2], Q[2][2]); GAP(); A.l[0] += p0[5]; PIN(A.l[0]); GAP();
  mfma_s(S2[1], A.kf[1][3], Q[2][3]); GAP(); A.l[0] += p0[6]; PIN(A.l[0]); GAP();
  asm volatile("" : "+v"(vlo[0]), "+v"(vhi[0]), "+v"(vlo[1]), "+v"(vhi[1]), "+v"(vlo[2]), "+v"(vhi[2]), "+v"(vlo[3]), "+v"(vhi[3]), "+v"(vlo[4]), "+v"(vhi[4]), "+v"(vlo[5]), "+v"(vhi[5]), "+v"(vlo[6]), "+v"(vhi[6]), "+v"(vlo[7]), "+v"(vhi[7])); A.vf[0] = __builtin_shufflevector(vlo[0], vhi[0], 0, 1, 2, 3, 4, 5, 6, 7); A.vf[1] = __builtin_shufflevector(vlo[1], vhi[1], 0, 1, 2, 3, 4, 5, 6, 7); A.vf[2] = __builtin_shufflevector(vlo[2], vhi[2], 0, 1, 2, 3, 4, 5, 6, 7); A.vf[3] = __builtin_shufflevector(vlo[3], vhi[3], 0, 1, 2, 3, 4, 5, 6, 7); A.vf[4] = __builtin_shufflevector(vlo[4], vhi[4], 0, 1, 2, 3, 4, 5, 6, 7); A.vf[5] = __builtin_shufflevector(vlo[5], vhi[5], 0, 1, 2, 3, 4, 5, 6, 7); A.vf[6] = __builtin_shufflevector(vlo[6], vhi[6], 0, 1, 2, 3, 4, 5, 6, 7); A.vf[7] = __builtin_shufflevector(vlo[7], vhi[7], 0, 1, 2, 3, 4, 5, 6, 7); mfma_acc<0>(A.vf[0], asb(P0)); GAP(); A.l[0] += p0[7]; PIN(A.l[0]); GAP();
  mfma_acc<4>(A.vf[1], asb(P0)); GAP(); p1[0] = __builtin_amdgcn_exp2f(S1[0][0]); GAP();
  mfma_acc<8>(A.vf[2], asb(P0)); GAP(); p1[1] = __builtin_amdgcn_exp2f(S1[0][1]); GAP();
  mfma_acc<12>(A.vf[3], asb(P0)); GAP(); p1[2] = __builtin_amdgcn_exp2f(S1[0][2]); GAP();
  mfma_acc<16>(A.vf[4], asb(P0)); GAP(); p1[3] = __builtin_amdgcn_exp2f(S1[0][3]); GAP();
  mfma_acc<20>(A.vf[5], asb(P0)); GAP(); P1[0] = cvtpk(p1[0], p1[1]); GAP(); A.l[1] += p1[0]; PIN(A.l[1]); GAP();
  mfma_acc<24>(A.vf[6], asb(P0)); GAP(); p1[4] = __builtin_amdgcn_exp2f(S1[1][0]); GAP();
  mfma_acc<28>(A.vf[7], asb(P0)); GAP(); p1[5] = __builtin_amdgcn_exp2f(S1[1][1]); GAP();
  mfma_s0(S0[0], A.kf[0][0], Q[3][0], A.negm[3]); GAP(); P1[1] = cvtpk(p1[2], p1[3]); GAP(); A.l[1] += p1[1]; PIN(A.l[1]); GAP();
  mfma_s(S0[0], A.kf[0][1], Q[3][1]); GAP(); p1[6] = __builtin_amdgcn_exp2f(S1[1][2]); GAP();
  mfma_s(S0[0], A.kf[0][2], Q[3][2]); GAP(); p1[7] = __builtin_amdgcn_exp2f(S1[1][3]); GAP();
  mfma_s(S0[0], A.kf[0][3], Q[3][3]); GAP(); P1[2] = cvtpk(p1[4], p1[5]); GAP(); A.l[1] += p1[2]; PIN(A.l[1]); GAP();
  mfma_s0(S0[1], A.kf[1][0], Q[3][0], A.negm[3]); GAP(); P1[3] = cvtpk(p1[6], p1[7]); PIN(P1); GAP(); A.l[1] += p1[3]; PIN(A.l[1]); GAP();
  mfma_s(S0[1], A.kf[1][1], Q[3][1]); GAP(); A.l[1] += p1[4]; PIN(A.l[1]); GAP();
  mfma_s(S0[1], A.kf[1][2], Q[3][2]); GAP(); A.l[1] += p1[5]; PIN(A.l[1]); GAP();
  mfma_s(S0[1], A.kf[1][3], Q[3][3]); GAP(); A.l[1] += p1[6]; PIN(A.l[1]); GAP();
  mfma_acc<32>(A.vf[0], asb(P1)); GAP(); A.l[1] += p1[7]; PIN(A.l[1]); GAP();
  mfma_acc<36>(A.vf[1], asb(P1)); GAP(); p0[0] = __builtin_amdgcn_exp2f(S2[0][0]); GAP();
  mfma_acc<40>(A.vf[2], asb(P1)); GAP(); p0[1] = __builtin_amdgcn_exp2f(S2[0][1]); GAP(); A.l[2] += p0[0]; PIN(A.l[2]); GAP();
  mfma_acc<44>(A.vf[3], asb(P1)); GAP(); p0[2] = __builtin_amdgcn_exp2f(S2[0][2]); GAP(); P0[0] = cvtpk(p0[0], p0[1]); GAP();
  mfma_acc<48>(A.vf[4], asb(P1)); GAP(); p0[3] = __builtin_amdgcn_exp2f(S2[0][3]); GAP(); A.l[2] += p0[1]; PIN(A.l[2]); GAP();
  mfma_acc<52>(A.vf[5], asb(P1)); GAP(); p0[4] = __builtin_amdgcn_exp2f(S2[1][0]); GAP();
  mfma_acc<56>(A.vf[6], asb(P1)); GAP(); p0[5] = __builtin_amdgcn_exp2f(S2[1][1]); GAP(); P0[1] = cvtpk(p0[2], p0[3]); GAP();
  mfma_acc<60>(A.vf[7], asb(P1)); GAP(); p0[6] = __builtin_amdgcn_exp2f(S2[1][2]); GAP();
  mfma_s0(S1[0], A.kf[0][0], Q[4][0], A.negm[4]); GAP(); p0[7] = __builtin_amdgcn_exp2f(S2[1][3]); GAP(); P0[2] = cvtpk(p0[4], p0[5]); GAP();
  mfma_s(S1[0], A.kf[0][1], Q[4][1]); GAP(); const unsigned kslot = (A.vslot + SLOT) & (3u * SLOT); GAP();
  mfma_s(S1[0], A.kf[0][2], Q[4][2]); GAP(); A.l[2] += p0[2]; PIN(A.l[2]); GAP(); P0[3] = cvtpk(p0[6], p0[7]); PIN(P0); GAP();
  mfma_s(S1[0], A.kf[0][3], Q[4][3]); GAP(); A.l[2] += p0[3]; PIN(A.l[2]); GAP(); p1[0] = __builtin_amdgcn_exp2f(S0[0][0]); GAP();
  mfma_s0(S1[1], A.kf[1][0], Q[4][0], A.negm[4]); GAP(); A.l[2] += p0[4]; PIN(A.l[2]); GAP(); p1[1] = __builtin_amdgcn_exp2f(S0[0][1]); GAP();
  mfma_s(S1[1], A.kf[1][1], Q[4][1]); GAP(); A.l[2] += p0[5]; PIN(A.l[2]); GAP(); P1[0] = cvtpk(p1[0], p1[1]); GAP();
  mfma_s(S1[1], A.kf[1][2], Q[4][2]); GAP(); A.l[2] += p0[6]; PIN(A.l[2]); GAP(); p1[2] = __builtin_amdgcn_exp2f(S0[0][2]); GAP();
  mfma_s(S1[1], A.kf[1][3], Q[4][3]); GAP(); const unsigned kb0 = c.kb_l + kslot, kb1 = kb0 ^ 16u, kb2 = kb0 ^ 32u, kb3 = kb0 ^ 48u; GAP();
  BSTEP(5); GAP(); mfma_acc<64>(A.vf[0], asb(P0)); GAP(); A.l[2] += p0[7]; PIN(A.l[2]); GAP(); A.kf[0][0] = *LDS8(kb0 + 0u); GAP();
  mfma_acc<68>(A.vf[1], asb(P0)); GAP(); A.kf[0][1] = *LDS8(kb1 + 0u); GAP(); A.kf[0][2] = *LDS8(kb2 + 0u); GAP();
  mfma_acc<72>(A.vf[2], asb(P0)); GAP(); A.kf[0][3] = *LDS8(kb3 + 0u); GAP(); A.kf[1][0] = *LDS8(kb0 + 4096u); GAP();
  mfma_acc<76>(A.vf[3], asb(P0)); GAP(); A.kf[1][1] = *LDS8(kb1 + 4096u); GAP(); A.kf[1][2] = *LDS8(kb2 + 4096u); GAP(); A.kf[1][3] = *LDS8(kb3 + 4096u); GAP();
  mfma_acc<80>(A.vf[4], asb(P0)); GAP(); p1[3] = __builtin_amdgcn_exp2f(S0[0][3]); GAP();
  mfma_acc<84>(A.vf[5], asb(P0)); GAP(); p1[4] = __builtin_amdgcn_exp2f(S0[1][0]); GAP();
  mfma_acc<88>(A.vf[6], asb(P0)); GAP(); p1[5] = __builtin_amdgcn_exp2f(S0[1][1]); GAP(); A.l[3] += p1[0]; PIN(A.l[3]); GAP();
  mfma_acc<92>(A.vf[7], asb(P0)); GAP(); P1[1] = cvtpk(p1[2], p1[3]); GAP(); A.l[3] += p1[1]; PIN(A.l[3]); GAP();
  asm volatile("" : "+v"(A.kf[0][0]), "+v"(A.kf[0][1]), "+v"(A.kf[0][2]), "+v"(A.kf[0][3]), "+v"(A.kf[1][0]), "+v"(A.kf[1][1]), "+v"(A.kf[1][2]), "+v"(A.kf[1][3])); mfma_s0(A.S[0], A.kf[0][0], Q[0][0], A.negm[0]); GAP(); p1[6] = __builtin_amdgcn_exp2f(S0[1][2]); GAP(); P1[2] = cvtpk(p1[4], p1[5]); GAP();
  mfma_s(A.S[0], A.kf[0][1], Q[0][1]); GAP(); p1[7] = __builtin_amdgcn_exp2f(S0[1][3]); GAP();
  mfma_s(A.S[0], A.kf[0][2], Q[0][2]); GAP(); A.l[3] += p1[2]; PIN(A.l[3]); GAP(); P1[3] = cvtpk(p1[6], p1[7]); PIN(P1); GAP();
  mfma_s(A.S[0], A.kf[0][3], Q[0][3]); GAP(); p0[0] = __builtin_amdgcn_exp2f(S1[0][0]); GAP(); A.l[3] += p1[3]; PIN(A.l[3]); GAP();
  mfma_s0(A.S[1], A.kf[1][0], Q[0][0], A.negm[0]); GAP(); p0[1] = __builtin_amdgcn_exp2f(S1[0][1]); GAP(); A.l[3] += p1[4]; PIN(A.l[3]); GAP();
  mfma_s(A.S[1], A.kf[1][1], Q[0][1]); GAP(); A.l[4] += p0[0]; PIN(A.l[4]); GAP(); A.l[3] += p1[5]; PIN(A.l[3]); GAP();
  mfma_s(A.S[1], A.kf[1][2], Q[0][2]); GAP(); p0[2] = __builtin_amdgcn_exp2f(S1[0][2]); GAP(); A.l[4] += p0[1]; PIN(A.l[4]); GAP();
  mfma_s(A.S[1], A.kf[1][3], Q[0][3]); GAP(); p0[3] = __builtin_amdgcn_exp2f(S1[0][3]); GAP();
  mfma_acc<96>(A.vf[0], asb(P1)); GAP(); A.l[3] += p1[6]; PIN(A.l[3]); GAP(); A.l[4] += p0[2]; PIN(A.l[4]); GAP(); A.P[0] = cvtpk(p0[0], p0[1]); GAP();
  mfma_acc<100>(A.vf[1], asb(P1)); GAP(); p0[4] = __builtin_amdgcn_exp2f(S1[1][0]); GAP();
  mfma_acc<104>(A.vf[2], asb(P1)); GAP(); A.l[3] += p1[7]; PIN(A.l[3]); GAP(); A.l[4] += p0[3]; PIN(A.l[4]); GAP(); A.P[1] = cvtpk(p0[2], p0[3]); GAP();
  mfma_acc<108>(A.vf[3], asb(P1)); GAP(); p0[5] = __builtin_amdgcn_exp2f(S1[1][1]); GAP();
  mfma_acc<112>(A.vf[4], asb(P1)); GAP(); A.l[4] += p0[4]; PIN(A.l[4]); GAP(); p0[6] = __builtin_amdgcn_exp2f(S1[1][2]); GAP();
  mfma_acc<116>(A.vf[5], asb(P1)); GAP(); A.l[4] += p0[5]; PIN(A.l[4]); GAP(); A.P[2] = cvtpk(p0[4], p0[5]); GAP();
  mfma_acc<120>(A.vf[6], asb(P1)); GAP(); p0[7] = __builtin_amdgcn_exp2f(S1[1][3]); GAP(); A.l[4] += p0[6]; PIN(A.l[4]); GAP();
  mfma_acc<124>(A.vf[7], asb(P1)); GAP(); A.P[3] = cvtpk(p0[6], p0[7]); PIN(A.P); GAP(); A.l[4] += p0[7]; PIN(A.l[4]); GAP();
  A.vslot = kslot; A.s += 1u;
}
template <> DI void astep_gen<4, K_FAST>(const Ctx& c, Att<4>& A, const u32x4 (&Q)[4][4]) {
  f32x4 S0[2], S1[2], S2[2]; S0[0] = A.S[0]; S0[1] = A.S[1]; float p0[8], p1[8]; u32x4 P0, P1; bf16x4 vlo[8], vhi[8];
  mfma_s0(S1[0], A.kf[0][0], Q[1][0], A.negm[1]); GAP(); const unsigned vb0 = c.vb_l + A.vslot, vb1 = vb0 ^ 32u; GAP();
  mfma_s(S1[0], A.kf[0][1], Q[1][1]); GAP(); p0[0] = __builtin_amdgcn_exp2f(S0[0][0]); GAP();
  mfma_s(S1[0], A.kf[0][2], Q[1][2]); GAP(); p0[1] = __builtin_amdgcn_exp2f(S0[0][1]); GAP(); A.l[0] += p0[0]; PIN(A.l[0]); GAP();
  mfma_s(S1[0], A.kf[0][3], Q[1][3]); GAP(); p0[2] = __builtin_amdgcn_exp2f(S0[0][2]); GAP(); P0[0] = cvtpk(p0[0], p0[1]); GAP();
  mfma_s0(S1[1], A.kf[1][0], Q[1][0], A.negm[1]); GAP(); p0[3] = __builtin_amdgcn_exp2f(S0[0][3]); GAP(); A.l[0] += p0[1]; PIN(A.l[0]); GAP();
  mfma_s(S1[1], A.kf[1][1], Q[1][1]); GAP(); p0[4] = __builtin_amdgcn_exp2f(S0[1][0]); GAP(); P0[1] = cvtpk(p0[2], p0[3]); GAP();
  mfma_s(S1[1], A.kf[1][2], Q[1][2]); GAP(); p0[5] = __builtin_amdgcn_exp2f(S0[1][1]); GAP(); A.l[0] += p0[2]; PIN(A.l[0]); GAP();
  mfma_s(S1[1], A.kf[1][3], Q[1][3]); GAP(); p0[6] = __builtin_amdgcn_exp2f(S0[1][2]); GAP(); P0[2] = cvtpk(p0[4], p0[5]); GAP();
  mfma_acc<96>(A.vf[0], asb(A.P)); GAP(); vlo[0] = __builtin_amdgcn_ds_read_tr16_b64_v4bf16(LDS4(vb0 + 0u)); GAP(); A.l[0] += p0[3]; PIN(A.l[0]); GAP(); vhi[0] = __builtin_amdgcn_ds_read_tr16_b64_v4bf16(LDS4(vb0 + 4096u)); GAP();
  mfma_acc<100>(A.vf[1], asb(A.P)); GAP(); vlo[1] = __builtin_amdgcn_ds_read_tr16_b64_v4bf16(LDS4(vb1 + 0u)); GAP(); A.l[0] += p0[4]; PIN(A.l[0]); GAP(); vhi[1] = __builtin_amdgcn_ds_read_tr16_b64_v4bf16(LDS4(vb1 + 4096u)); GAP();
  mfma_acc<104>(A.vf[2], asb(A.P)); GAP(); vlo[2] = __builtin_amdgcn_ds_read_tr16_b64_v4bf16(LDS4(vb0 + 512u)); GAP(); A.l[0] += p0[5]; PIN(A.l[0]); GAP(); vhi[2] = __builtin_amdgcn_ds_read_tr16_b64_v4bf16(LDS4(vb0 + 4608u)); GAP();
  mfma_acc<108>(A.vf[3], asb(A.P)); GAP(); vlo[3] = __builtin_amdgcn_ds_read_tr16_b64_v4bf16(LDS4(vb1 + 512u)); GAP(); A.l[0] += p0[6]; PIN(A.l[0]); GAP(); vhi[3] = __builtin_amdgcn_ds_read_tr16_b64_v4bf16(LDS4(vb1 + 4608u)); GAP();
  mfma_acc<112>(A.vf[4], asb(A.P)); GAP(); vlo[4] = __builtin_amdgcn_ds_read_tr16_b64_v4bf16(LDS4(vb0 + 1024u)); GAP(); vhi[4] = __builtin_amdgcn_ds_read_tr16_b64_v4bf16(LDS4(vb0 + 5120u)); GAP();
  mfma_acc<116>(A.vf[5], asb(A.P)); GAP(); vlo[5] = __builtin_amdgcn_ds_read_tr16_b64_v4bf16(LDS4(vb1 + 1024u)); GAP(); vhi[5] = __builtin_amdgcn_ds_read_tr16_b64_v4bf16(LDS4(vb1 + 5120u)); GAP();
  mfma_acc<120>(A.vf[6], asb(A.P)); GAP(); vlo[6] = __builtin_amdgcn_ds_read_tr16_b64_v4bf16(LDS4(vb0 + 1536u)); GAP(); vhi[6] = __builtin_amdgcn_ds_read_tr16_b64_v4bf16(LDS4(vb0 + 5632u)); GAP();
  mfma_acc<124>(A.vf[7], asb(A.P)); GAP(); vlo[7] = __builtin_amdgcn_ds_read_tr16_b64_v4bf16(LDS4(vb1 + 1536u)); GAP(); vhi[7] = __builtin_amdgcn_ds_read_tr16_b64_v4bf16(LDS4(vb1 + 5632u)); GAP();
  mfma_s0(S2[0], A.kf[0][0], Q[2][0], A.negm[2]); GAP(); p0[7] = __builtin_amdgcn_exp2f(S0[1][3]); GAP();
  mfma_s(S2[0], A.kf[0][1], Q[2][1]); GAP(); P0[3] = cvtpk(p0[6], p0[7]); PIN(P0); GAP(); A.l[0] += p0[7]; PIN(A.l[0]); GAP();
  mfma_s(S2[0], A.kf[0][2], Q[2][2]); GAP(); p1[0] = __builtin_amdgcn_exp2f(S1[0][0]); GAP();
  mfma_s(S2[0], A.kf[0][3], Q[2][3]); GAP(); p1[1] = __builtin_amdgcn_exp2f(S1[0][1]); GAP(); A.l[1] += p1[0]; PIN(A.l[1]); GAP();
  mfma_s0(S2[1], A.kf[1][0], Q[2][0], A.negm[2]); GAP(); p1[2] = __builtin_amdgcn_exp2f(S1[0][2]); GAP(); P1[0] = cvtpk(p1[0], p1[1]); GAP();
  mfma_s(S2[1], A.kf[1][1], Q[2][1]); GAP(); p1[3] = __builtin_amdgcn_exp2f(S1[0][3]); GAP(); A.l[1] += p1[1]; PIN(A.l[1]); GAP();
  mfma_s(S2[1], A.kf[1][2], Q[2][2]); GAP(); p1[4] = __builtin_amdgcn_exp2f(S1[1][0]); GAP(); P1[1] = cvtpk(p1[2], p1[3]); GAP();
  mfma_s(S2[1], A.kf[1][3], Q[2][3]); GAP(); p1[5] = __builtin_amdgcn_exp2f(S1[1][1]); GAP(); A.l[1] += p1[2]; PIN(A.l[1]); GAP();
  asm volatile("" : "+v"(vlo[0]), "+v"(vhi[0]), "+v"(vlo[1]), "+v"(vhi[1]), "+v"(vlo[2]), "+v"(vhi[2]), "+v"(vlo[3]), "+v"(vhi[3]), "+v"(vlo[4]), "+v"(vhi[4]), "+v"(vlo[5]), "+v"(vhi[5]), "+v"(vlo[6]), "+v"(vhi[6]), "+v"(vlo[7]), "+v"(vhi[7])); A.vf[0] = __builtin_shufflevector(vlo[0], vhi[0], 0, 1, 2, 3, 4, 5, 6, 7); A.vf[1] = __builtin_shufflevector(vlo[1], vhi[1], 0, 1, 2, 3, 4, 5, 6, 7); A.vf[2] = __builtin_shufflevector(vlo[2], vhi[2], 0, 1, 2, 3, 4, 5, 6, 7); A.vf[3] = __builtin_shufflevector(vlo[3], vhi[3], 0, 1, 2, 3, 4, 5, 6, 7); A.vf[4] = __builtin_shufflevector(vlo[4], vhi[4], 0, 1, 2, 3, 4, 5, 6, 7); A.vf[5] = __builtin_shufflevector(vlo[5], vhi[5], 0, 1, 2, 3, 4, 5, 6, 7); A.vf[6] = __builtin_shufflevector(vlo[6], vhi[6], 0, 1, 2, 3, 4, 5, 6, 7); A.vf[7] = __builtin_shufflevector(vlo[7], vhi[7], 0, 1, 2, 3, 4, 5, 6, 7); mfma_acc<0>(A.vf[0], asb(P0)); GAP(); p1[6] = __builtin_amdgcn_exp2f(S1[1][2]); GAP(); P1[2] = cvtpk(p1[4], p1[5]); GAP();
  mfma_acc<4>(A.vf[1], asb(P0)); GAP(); p1[7] = __builtin_amdgcn_exp2f(S1[1][3]); GAP(); A.l[1] += p1[3]; PIN(A.l[1]); GAP();
  mfma_acc<8>(A.vf[2], asb(P0)); GAP(); P1[3] = cvtpk(p1[6], p1[7]); PIN(P1); GAP(); A.l[1] += p1[4]; PIN(A.l[1]); GAP();
  mfma_acc<12>(A.vf[3], asb(P0)); GAP(); p0[0] = __builtin_amdgcn_exp2f(S2[0][0]); GAP(); A.l[1] += p1[5]; PIN(A.l[1]); GAP();
  mfma_acc<16>(A.vf[4], asb(P0)); GAP(); p0[1] = __builtin_amdgcn_exp2f(S2[0][1]); GAP(); A.l[1] += p1[6]; PIN(A.l[1]); GAP();
  mfma_acc<20>(A.vf[5], asb(P0)); GAP(); p0[2] = __builtin_amdgcn_exp2f(S2[0][2]); GAP(); A.l[1] += p1[7]; PIN(A.l[1]); GAP();
  mfma_acc<24>(A.vf[6], asb(P0)); GAP(); p0[3] = __builtin_amdgcn_exp2f(S2[0][3]); GAP(); A.l[2] += p0[0]; PIN(A.l[2]); GAP();
  mfma_acc<28>(A.vf[7], asb(P0)); GAP(); p0[4] = __builtin_amdgcn_exp2f(S2[1][0]); GAP(); A.l[2] += p0[1]; PIN(A.l[2]); GAP();
  mfma_s0(S0[0], A.kf[0][0], Q[3][0], A.negm[3]); GAP(); P0[0] = cvtpk(p0[0], p0[1]); GAP(); p0[5] = __builtin_amdgcn_exp2f(S2[1][1]); GAP();
  mfma_s(S0[0], A.kf[0][1], Q[3][1]); GAP(); P0[1] = cvtpk(p0[2], p0[3]); GAP(); p0[6] = __builtin_amdgcn_exp2f(S2[1][2]); GAP();
  mfma_s(S0[0], A.kf[0][2], Q[3][2]); GAP(); p0[7] = __builtin_amdgcn_exp2f(S2[1][3]); GAP(); P0[2] = cvtpk(p0[4], p0[5]); GAP();
  mfma_s(S0[0], A.kf[0][3], Q[3][3]); GAP(); A.l[2] += p0[2]; PIN(A.l[2]); GAP(); P0[3] = cvtpk(p0[6], p0[7]); PIN(P0); GAP();
  mfma_s0(S0[1], A.kf[1][0], Q[3][0], A.negm[3]); GAP(); A.l[2] += p0[3]; PIN(A.l[2]); GAP();
  mfma_s(S0[1], A.kf[1][1], Q[3][1]); GAP(); const unsigned kslot = (A.vslot + SLOT) & (3u * SLOT); const unsigned m0n = c.m0w + A.vslot; const unsigned son = kv_so(A.s + 4u, A.lim), so2 = son + 4096u; GAP();
  mfma_s(S0[1], A.kf[1][2], Q[3][2]); GAP(); A.l[2] += p0[4]; PIN(A.l[2]); GAP();
  mfma_s(S0[1], A.kf[1][3], Q[3][3]); GAP(); const unsigned kb0 = c.kb_l + kslot, kb1 = kb0 ^ 16u, kb2 = kb0 ^ 32u, kb3 = kb0 ^ 48u; GAP();
  BSTEP(4); GAP(); mfma_acc<32>(A.vf[0], asb(P1)); GAP(); A.kf[0][0] = *LDS8(kb0 + 0u); GAP(); A.kf[0][1] = *LDS8(kb1 + 0u); GAP(); A.kf[0][2] = *LDS8(kb2 + 0u); GAP();
  mfma_acc<36>(A.vf[1], asb(P1)); GAP(); A.kf[0][3] = *LDS8(kb3 + 0u); GAP(); A.kf[1][0] = *LDS8(kb0 + 4096u); GAP(); A.kf[1][1] = *LDS8(kb1 + 4096u); GAP();
  mfma_acc<40>(A.vf[2], asb(P1)); GAP(); A.kf[1][2] = *LDS8(kb2 + 4096u); GAP(); A.kf[1][3] = *LDS8(kb3 + 4096u); GAP(); kv_m0<0>(m0n); GAP();
  mfma_acc<44>(A.vf[3], asb(P1)); GAP(); kv_dma<0, 0>(c, son, so2); GAP(); p1[0] = __builtin_amdgcn_exp2f(S0[0][0]); GAP();
  mfma_acc<48>(A.vf[4], asb(P1)); GAP(); kv_dma<0, 1>(c, son, so2); GAP(); A.l[3] += p1[0]; PIN(A.l[3]); GAP(); kv_m0<1>(m0n); GAP();
  mfma_acc<52>(A.vf[5], asb(P1)); GAP(); kv_dma<1, 0>(c, son, so2); GAP(); p1[1] = __builtin_amdgcn_exp2f(S0[0][1]); GAP();
  mfma_acc<56>(A.vf[6], asb(P1)); GAP(); kv_dma<1, 1>(c, son, so2); GAP(); A.l[2] += p0[5]; PIN(A.l[2]); GAP(); kv_m0<2>(m0n); GAP();
  mfma_acc<60>(A.vf[7], asb(P1)); GAP(); kv_dma<2, 0>(c, son, so2); GAP(); A.l[3] += p1[1]; PIN(A.l[3]); GAP();
  asm volatile("" : "+v"(A.kf[0][0]), "+v"(A.kf[0][1]), "+v"(A.kf[0][2]), "+v"(A.kf[0][3]), "+v"(A.kf[1][0]), "+v"(A.kf[1][1]), "+v"(A.kf[1][2]), "+v"(A.kf[1][3])); mfma_s0(A.S[0], A.kf[0][0], Q[0][0], A.negm[0]); GAP(); kv_dma<2, 1>(c, son, so2); GAP(); A.l[2] += p0[6]; PIN(A.l[2]); GAP(); kv_m0<3>(m0n); GAP();
  mfma_s(A.S[0], A.kf[0][1], Q[0][1]); GAP(); kv_dma<3, 0>(c, son, so2); GAP(); A.l[2] += p0[7]; PIN(A.l[2]); GAP();
  mfma_s(A.S[0], A.kf[0][2], Q[0][2]); GAP(); kv_dma<3, 1>(c, son, so2); GAP(); kv_m0<4>(m0n); GAP(); A.P[0] = cvtpk(p1[0], p1[1]); GAP();
  mfma_s(A.S[0], A.kf[0][3], Q[0][3]); GAP(); kv_dma<4, 0>(c, son, so2); GAP();
  mfma_s0(A.S[1], A.kf[1][0], Q[0][0], A.negm[0]); GAP(); p1[2] = __builtin_amdgcn_exp2f(S0[0][2]); GAP(); kv_dma<4, 1>(c, son, so2); GAP();
  mfma_s(A.S[1], A.kf[1][1], Q[0][1]); GAP(); kv_m0<5>(m0n); GAP(); p1[3] = __builtin_amdgcn_exp2f(S0[0][3]); GAP();
  mfma_s(A.S[1], A.kf[1][2], Q[0][2]); GAP(); A.l[3] += p1[2]; PIN(A.l[3]); GAP(); kv_dma<5, 0>(c, son, so2); GAP(); A.P[1] = cvtpk(p1[2], p1[3]); GAP();
  mfma_s(A.S[1], A.kf[1][3], Q[0][3]); GAP(); p1[4] = __builtin_amdgcn_exp2f(S0[1][0]); GAP();
  mfma_acc<64>(A.vf[0], asb(P0)); GAP(); A.l[3] += p1[3]; PIN(A.l[3]); GAP(); kv_dma<5, 1>(c, son, so2); GAP(); kv_m0<6>(m0n); GAP();
  mfma_acc<68>(A.vf[1], asb(P0)); GAP(); p1[5] = __builtin_amdgcn_exp2f(S0[1][1]); GAP();
  mfma_acc<72>(A.vf[2], asb(P0)); GAP(); A.l[3] += p1[4]; PIN(A.l[3]); GAP(); kv_dma<6, 0>(c, son, so2); GAP(); A.P[2] = cvtpk(p1[4], p1[5]); GAP();
  mfma_acc<76>(A.vf[3], asb(P0)); GAP(); p1[6] = __builtin_amdgcn_exp2f(S0[1][2]); GAP();
  mfma_acc<80>(A.vf[4], asb(P0)); GAP(); A.l[3] += p1[5]; PIN(A.l[3]); GAP(); kv_dma<6, 1>(c, son, so2); GAP(); kv_m0<7>(m0n); GAP();
  mfma_acc<84>(A.vf[5], asb(P0)); GAP(); p1[7] = __builtin_amdgcn_exp2f(S0[1][3]); GAP();
  mfma_acc<88>(A.vf[6], asb(P0)); GAP(); A.l[3] += p1[6]; PIN(A.l[3]); GAP(); kv_dma<7, 0>(c, son, so2); GAP(); A.P[3] = cvtpk(p1[6], p1[7]); PIN(A.P); GAP();
  mfma_acc<92>(A.vf[7], asb(P0)); GAP(); A.l[3] += p1[7]; PIN(A.l[3]); GAP(); kv_dma<7, 1>(c, son, so2); GAP();
  A.vslot = kslot; A.s += 1u;
}

template <int NB>
DI void astep_safe(const Ctx& c, Att<NB>& A, const u32x4 (&Q)[NB][4]) {
  const unsigned s = A.s;
  const unsigned vb0 = c.vb_l + (s & 3u) * SLOT, vb1 = vb0 ^ 32u;
  const unsigned kb0 = c.kb_l + ((s + 1u) & 3u) * SLOT;
  sfor<NB>([&](auto B) {
    constexpr int b = B, nb = (b + 1) % NB, pb = (b + NB - 1) % NB;
    f32x4 Sn[2];
    float ts = 0.f;
    sfor<16>([&](auto G) {
      constexpr int g = G;
      if constexpr (b == NB - 2 && g == 8) {
        bstep<NB>();
        sfor<8>([&](auto I) { A.kf[I >> 2][I & 3] = *LDS8((kb0 ^ (16u * (I & 3))) + 4096u * (I >> 2)); });
        if constexpr (NB == 4) dma_kv(c, s + 4u, A.lim);
        GAP();
      }
      if constexpr (g < 8) {
        constexpr int k2 = g >> 2, ss = g & 3;
        if constexpr (ss == 0) mfma_s0(Sn[k2], A.kf[k2][0], Q[nb][0], A.negm[nb]); else mfma_s(Sn[k2], A.kf[k2][ss], Q[nb][ss]);
      } else {
        constexpr int t = g - 8;
        mfma_acc<32 * pb + 4 * t>(A.vf[t], asb(A.P));
        if constexpr (b == 0) {
          const unsigned a = ((t & 1) ? vb1 : vb0) + 512u * (t >> 1);
          bf16x4 lo = __builtin_amdgcn_ds_read_tr16_b64_v4bf16(LDS4(a));
          bf16x4 hi = __builtin_amdgcn_ds_read_tr16_b64_v4bf16(LDS4(a + 4096u));
          A.vf[t] = __builtin_shufflevector(lo, hi, 0, 1, 2, 3, 4, 5, 6, 7);
        }
      }
      if constexpr (g < 2) {
        A.P[2 * g] = cvtpk(A.p[4 * g], A.p[4 * g + 1]); A.P[2 * g + 1] = cvtpk(A.p[4 * g + 2], A.p[4 * g + 3]);
        if constexpr (g == 1) PIN(A.P);
      }
      if constexpr (g == 4) A.l[pb] += A.tsp;
      if constexpr (g >= 2 && g < 14) {
        constexpr int q = (g - 2) / 3, r = (g - 2) % 3;
        if constexpr (r < 2) { constexpr int e = 2 * q + r; A.p[e] = __builtin_amdgcn_exp2f(A.S[e >> 2][e & 3]); }
        else { if constexpr (q == 0) ts = A.p[0] + A.p[1]; else { ts += A.p[2 * q]; ts += A.p[2 * q + 1]; } }
      }
      GAP();
    });
    if (__builtin_expect(__any(!(ts <= LTHR)), 0)) {
      float m8 = fmaxf(fmaxf(fmaxf(A.S[0][0], A.S[0][1]), fmaxf(A.S[0][2], A.S[0][3])), fmaxf(fmaxf(A.S[1][0], A.S[1][1]), fmaxf(A.S[1][2], A.S[1][3])));
      m8 = xmax32(xmax16(m8));
      const float dl = fmaxf(m8, 0.f);
      const float f = __builtin_amdgcn_exp2f(-dl);
      A.l[b] *= f;
      sfor<32>([&](auto I) { const float v = acc_rd<32 * b + I>(); acc_wr<32 * b + I>(__float_as_uint(v * f)); });
      A.negm[b] -= dl;
      ts = 0.f;
      sfor<8>([&](auto E) { A.p[E] = __builtin_amdgcn_exp2f(A.S[E >> 2][E & 3] - dl); ts += A.p[E]; });
      asm volatile("s_nop 1" : "+v"(A.negm[b]));
    }
    A.tsp = ts;
    A.S[0] = Sn[0]; A.S[1] = Sn[1];
    GAP();
  });
  A.s += 1u;
}

template <int NB, bool SAFE>
DI bool attention(const Ctx& c, unsigned round, unsigned lim, bool cont, const u32x4 (&Q)[NB][4], float (&il)[NB]) {
  Att<NB> A;
  const unsigned s0 = round * 16u;
  A.vslot = 0; A.s = s0; A.lim = lim;
  if (!cont) wait_bar<(NB == 4) ? (SAFE ? 32 : 63) : 0>();
  sfor<8>([&](auto I) { A.kf[I >> 2][I & 3] = *LDS8((c.kb_l ^ (16u * (I & 3))) + 4096u * (I >> 2)); });
  if constexpr (NB == 4) { if (!cont) dma_kv(c, s0 + 3u, lim); }
  A.P = u32x4{0u, 0u, 0u, 0u};
  sfor<8>([&](auto T) { A.vf[T] = asb(A.P); });
  if constexpr (SAFE) { sfor<8>([&](auto T) { A.p[T] = 0.f; }); A.tsp = 0.f; }
  asm volatile("s_nop 1" : "+v"(A.P));
  GAP();
  f32x4 S0[NB];
  sfor<NB>([&](auto B) { sfor<4>([&](auto SS) { if constexpr (SS == 0) mfma_v0(S0[B], A.kf[0][0], asb(Q[B][0])); else mfma_s(S0[B], A.kf[0][SS], Q[B][SS]); }); });
  sfor<2 * NB>([&](auto I) { zero16<16 * I>(asb(A.P)); });
  GAP();
  sfor<NB>([&](auto B) {
    const float mx = xmax32(xmax16(max2f(max3f(S0[B][0], S0[B][1], S0[B][2]), S0[B][3])));
    A.negm[B] = f32x4{-mx, -mx, -mx, -mx}; A.l[B] = 0.f;
  });
  asm volatile("s_nop 1" : "+v"(A.negm[0]));
  GAP();
  sfor<8>([&](auto I) {
    constexpr int k2 = I >> 2, ss = I & 3;
    if constexpr (ss == 0) mfma_s0(A.S[k2], A.kf[k2][0], Q[0][0], A.negm[0]); else mfma_s(A.S[k2], A.kf[k2][ss], Q[0][ss]);
  });
  asm volatile("s_nop 5" ::: "memory");
  GAP();
  if constexpr (SAFE) {
    for (unsigned tt = 0; tt < 16; ++tt) astep_safe<NB>(c, A, Q);
    A.l[NB - 1] += A.tsp;
    A.P = u32x4{cvtpk(A.p[0], A.p[1]), cvtpk(A.p[2], A.p[3]), cvtpk(A.p[4], A.p[5]), cvtpk(A.p[6], A.p[7])};
  } else {
    for (unsigned tt = 0; tt < 8; ++tt) { astep_gen<NB, K_FAST>(c, A, Q); astep_gen<NB, K_FAST>(c, A, Q); }
  }
  asm volatile("s_nop 1" : "+v"(A.P));
  sfor<8>([&](auto T) { mfma_acc<32 * (NB - 1) + 4 * T>(A.vf[T], asb(A.P)); });
  bool bad = false;
  sfor<NB>([&](auto B) { const float ls = xsum4(A.l[B]); bad = bad || !(ls <= LBIG); il[B] = __builtin_amdgcn_rcpf(ls); });
  return bad;
}
template <int B, int S> DI u32x4 opack(float il) {
  u32x4 w;
  sfor<4>([&](auto M) {
    f32x2 v = {acc_rd<32 * B + 8 * S + 2 * M>(), acc_rd<32 * B + 8 * S + 2 * M + 1>()};
    v *= il;
    w[M.value] = cvtpk(v[0], v[1]);
  });
  PIN(w); GAP();
  return w;
}

template <int R, int OFF> DI void lds_wr_acc(unsigned addr) { asm volatile("ds_write_b128 %0, a[%c1:%c2] offset:%c3" :: "v"(addr), "i"(R), "i"(R + 3), "i"(OFF) : "memory"); }
template <int NB>
DI void front(Ctx& c, bool g2, const __amdgpu_buffer_rsrc_t& srdO, unsigned wsh, float (&il5)[5]) {
  const unsigned p = c.wid >> 1;
  u32x4 Q[NB][4];
  float il[NB];
  sfor<NB>([&](auto B) { il[B] = 0.f; sfor<4>([&](auto S) { Q[B][S] = u32x4{0u, 0u, 0u, 0u}; }); });
  typedef __attribute__((address_space(1))) unsigned* gu32;
  typedef __attribute__((address_space(3))) unsigned* ldsu_t;
  unsigned want = 0;
  if constexpr (NB == 5) {
    if (c.wid == 0) {
      const unsigned ln = lane_id();
      const unsigned f0 = __hip_atomic_load((gu32)(c.fw + (ln & 7u) * 64u), __ATOMIC_RELAXED, __HIP_MEMORY_SCOPE_AGENT);
      if (ln < 8u) {
        __hip_atomic_store((gu32)(c.fw + ln * 64u + 32u + c.hg), f0 + 0x5bd1e995u, __ATOMIC_RELAXED, __HIP_MEMORY_SCOPE_AGENT);
        *(ldsu_t)(size_t)(c.ldsb + FLAGS + 32u + ln * 4u) = f0;
      }
      want = __builtin_amdgcn_readlane(f0, c.hg) + 0x5bd1e995u;
    }
  }
  for (unsigned ph = 0; ph < 4; ++ph) {
    const bool isq = g2 ? (ph < 2) : ((ph & 1u) == 0);
    if (isq) {
      const unsigned r = g2 ? (ph ^ 1u) : (ph >> 1);
      const bool park = g2 && ph == 0;
      if constexpr (NB == 5) {
        if (ph != 0 && c.wid == 0) {
          gu32 aw = (gu32)(c.fw + c.hg * 64u + 32u + (lane_id() & 7u));
          for (unsigned spin = 0; spin < (1u << 21); ++spin) {
            if (__all(__hip_atomic_load(aw, __ATOMIC_RELAXED, __HIP_MEMORY_SCOPE_AGENT) == want)) break;
            __builtin_amdgcn_s_sleep(4);
          }
        }
      }
      qproj<NB>(c, (2u * r + p) * 256u, ph, ph == 1 ? 32u : ph * 8u, park ? 32u : (g2 ? 2u : r * 16u + 2u), Q);
      if (park) sfor<NB>([&](auto B) { sfor<4>([&](auto S) { sfor<4>([&](auto M) { acc_wr<AR + 16 * B + 4 * S + M>(Q[B][S][M.value]); }); }); });
    } else {
      const unsigned r = g2 ? (ph & 1u) : (ph >> 1);
      const unsigned lim = (g2 || r == 1) ? 32u : 16u;
      if constexpr (NB == 5) {
        if (r == 1) {
          const unsigned ln = lane_id();
          set_m0<XST>(c.ldsb);
          for (unsigned j = 0; j < 16; ++j) dma_any(srdO, ln * 16u, (wsh * 32u + p * 16u + j) * 1024u);
        }
      }
      const bool bad = attention<NB, false>(c, r, lim, g2 && r == 1, Q, il);
      ldsu_t flg = (ldsu_t)(size_t)(c.ldsb + FLAGS + r * 16u);
      flg[c.wid] = __any(bad) ? 1u : 0u;
      if (lim == 32u && r == 0) wait_bar<63>(); else wait_bar<0>();
      const u32x4 f4 = *(__attribute__((address_space(3))) u32x4*)flg;
      if (__builtin_expect(rfl(f4[0] | f4[1] | f4[2] | f4[3]) != 0u, 0)) {
        wait_bar<0>();
        if constexpr (NB == 4) { dma_kv(c, r * 16u, 32); dma_kv(c, r * 16u + 1u, 32); dma_kv(c, r * 16u + 2u, 32); }
        attention<NB, true>(c, r, lim, false, Q, il);
        wait_bar<0>();
      }
      asm volatile("s_nop 7" ::: "memory");
      if (r == 0) {
        if (g2) sfor<NB>([&](auto B) { sfor<4>([&](auto S) { sfor<4>([&](auto M) { Q[B][S][M.value] = __float_as_uint(acc_rd<AR + 16 * B + 4 * S + M>()); }); }); });
        else if constexpr (NB == 4) q_pre(c, (2u + p) * 256u);
        sfor<NB>([&](auto B) { sfor<4>([&](auto S) { const u32x4 w = opack<B, S>(il[B]); sfor<4>([&](auto M) { acc_wr<AR + 16 * B + 4 * S + M>(w[M.value]); }); }); });
      }
    }
  }
  sfor<5>([&](auto B) { if constexpr (B < NB) il5[B] = il[B]; else il5[B] = 0.f; });
}
template <int NB> DI void fin(const Ctx& c, unsigned ln, const float (&il)[5]) {
  const unsigned ob = c.ldsb + (c.wid >> 1) * 36864u + (NB == 4 ? 5120u : 0u) + ln * 16u;
  lds8_t o2 = LDS8(ob + 73728u);
  sfor<4>([&](auto S) { sfor<NB>([&](auto B) { lds_wr_acc<AR + 16 * B + 4 * S, (S * 9 + B) * 1024>(ob); o2[(S * 9 + B) * 64] = asb(opack<B, S>(il[B])); }); });
}

template <bool Z>
DI void ostep(const __amdgpu_buffer_rsrc_t& srd, unsigned so, unsigned ob, unsigned wo_l, u32x4 (&rc)[8], u32x4 (&rn)[8], u32x4 (&rl)[8], u32x4& wf0, bf16x8 (&of)[9], f32x4 (&a8)[8]) {
  u32x4 wf[2]; wf[0] = wf0;
  sfor<8>([&](auto T) {
    constexpr int t = T;
    if constexpr (t == 7) wait_raw<8>(rn);
    sfor<9>([&](auto B) {
      constexpr int b = B;
      if constexpr (Z) { if constexpr (b < 8) mfma_acc0<32 * b + 4 * t>(asb(wf[t & 1]), of[b]); else mfma_v0(a8[t], asb(wf[t & 1]), of[8]); }
      else { if constexpr (b < 8) mfma_acc<32 * b + 4 * t>(asb(wf[t & 1]), of[b]); else mfma_v(a8[t], asb(wf[t & 1]), of[8]); }
      GAP();
      if constexpr (t == 7) of[b] = *LDS8(ob + b * 1024u);
      if constexpr (b < 4) {
        if constexpr (t < 7) perm1<t + 1, b>(wf[(t + 1) & 1], rc); else perm1<0, b>(wf[0], rn);
        if constexpr (b == 3) PIN(wf[(t + 1) & 1]);
      }
      if constexpr (t < 4 && (b == 5 || b == 7)) { constexpr int j = 2 * t + (b == 7); ld_row_o<(j & 3) * 1024>(rl[j], wo_l, srd, so + (j >> 2) * 16384u); }
      GAP();
    });
  });
  wf0 = wf[0];
}

__global__ __launch_bounds__(256, 1) void fused_attn(const char* __restrict__ xA, const char* __restrict__ kA, const char* __restrict__ vA, const char* __restrict__ wqA, const char* __restrict__ woA,
                                                     const char* __restrict__ xB, const char* __restrict__ kB, const char* __restrict__ vB, const char* __restrict__ wqB, const char* __restrict__ woB,
                                                     char* __restrict__ part, unsigned* __restrict__ cnt, char* __restrict__ out) {
  extern __shared__ __attribute__((aligned(16))) char lds[];
  const unsigned tid = threadIdx.x, lane = tid & 63u, wid = rfl(tid >> 6);
  const unsigned xcd = blockIdx.x & 7u, idx = blockIdx.x >> 3, br = xcd >> 2;
  const unsigned bat = idx >> 1, hsel = idx & 1u;
  const unsigned hg = 2u * (xcd & 3u) + hsel, bb = br * 16u + bat;
  const bool g2 = (bat & 1u) != 0;
  const char* x = br ? xB : xA; const char* kc = br ? kB : kA; const char* vc = br ? vB : vA; const char* wq = br ? wqB : wqA; const char* wo = br ? woB : woA;
  Ctx c;
  c.wid = wid; c.ldsb = (unsigned)(size_t)lds; c.m0w = c.ldsb + (wid >> 1) * 16384u;
  const unsigned p = wid >> 1, isV = wid & 1u;
  c.rot = (bat + 2u * hg) & 15u;
  const size_t kvo = (size_t)(bat * 32u + hg * 4u + p) * 131072u;
  c.srdK = __builtin_amdgcn_make_buffer_rsrc((void*)(kc + kvo), (short)0, 393216, 0x00020000);
  c.srdV = __builtin_amdgcn_make_buffer_rsrc((void*)(vc + kvo), (short)0, 393216, 0x00020000);
  c.srdX = __builtin_amdgcn_make_buffer_rsrc((void*)(x + (size_t)bat * 147456u), (short)0, 147456, 0x00020000);
  c.srdW = __builtin_amdgcn_make_buffer_rsrc((void*)(wq + hg * 1024u), (short)0, (int)(4194304u - hg * 1024u), 0x00020000);
  const __amdgpu_buffer_rsrc_t srdO = __builtin_amdgcn_make_buffer_rsrc((void*)(wo + (size_t)hg * 524288u), (short)0, 524288, 0x00020000);
  {
    const unsigned r8 = lane >> 3;
    const unsigned vk = r8 * 256u + 16u * ((lane & 7u) ^ ((r8 >> 1) & 3u));
    const unsigned rv = (lane >> 2) & 7u;
    const unsigned vv = rv * 256u + 64u * (lane >> 5) + 16u * ((lane & 3u) ^ (2u * (rv >> 2)));
    c.vk2[0] = vk; c.vk2[1] = vk + 128u; c.vv2[0] = vv; c.vv2[1] = vv + 128u;
  }
  const unsigned i16 = lane & 15u, g = lane >> 4;
  c.kb_l = c.ldsb + p * 16384u + (i16 >> 3) * 2048u + (g >> 1) * 1024u + (i16 & 7u) * 128u + 64u * (g & 1u) + 16u * ((i16 >> 1) & 3u);
  {
    const unsigned qq = i16 >> 2, pp = lane & 3u;
    c.vb_l = c.ldsb + p * 16384u + 8192u + (g >> 1) * 2048u + (4u * (g & 1u) + qq) * 64u + 16u * ((pp >> 1) ^ (2u * (g & 1u))) + 8u * (pp & 1u);
  }
  typedef __attribute__((address_space(1))) unsigned* gu32;
  c.fw = cnt + bb * 512u; c.hg = hg;
  float il[5];
  if (isV) front<4>(c, g2, srdO, bat, il); else front<5>(c, g2, srdO, bat, il);
  const unsigned ln = lane_id();
  const unsigned wo_l = (ln >> 4) * 4096u + wid * 256u + (ln & 15u) * 16u;
  u32x4 r0[8], r1[8], r2[8];
  sfor<8>([&](auto J) { ld_row_o<(J & 3) * 1024>(r0[J], wo_l, srdO, (J >> 2) * 16384u); });
  sfor<8>([&](auto J) { ld_row_o<(J & 3) * 1024>(r1[J], wo_l, srdO, 32768u + (J >> 2) * 16384u); });
  const unsigned f0 = *(__attribute__((address_space(3))) unsigned*)(size_t)(c.ldsb + FLAGS + 32u + (ln & 7u) * 4u);
  if (isV) fin<4>(c, ln, il); else fin<5>(c, ln, il);
  wait_bar<63>();
  bf16x8 of[9];
  unsigned ob = c.ldsb + ln * 16u;
  sfor<9>([&](auto B) { of[B] = *LDS8(ob + B * 1024u); });
  f32x4 a8[8];
  wait_raw<8>(r0);
  u32x4 wf0;
  sfor<4>([&](auto M) { perm1<0, M>(wf0, r0); });
  asm volatile("s_nop 1" : "+v"(wf0));
  GAP();
  unsigned so = 65536u;
  ostep<true>(srdO, so, ob + 9216u, wo_l, r0, r1, r2, wf0, of, a8);
  for (unsigned it = 0; it < 4; ++it) {
    ostep<false>(srdO, so + 32768u, ob + 2u * 9216u, wo_l, r1, r2, r0, wf0, of, a8);
    ostep<false>(srdO, so + 2u * 32768u, ob + 3u * 9216u, wo_l, r2, r0, r1, wf0, of, a8);
    ostep<false>(srdO, so + 3u * 32768u, ob + 4u * 9216u, wo_l, r0, r1, r2, wf0, of, a8);
    so += 3u * 32768u; ob += 3u * 9216u;
  }
  u32x4 F[16];
  bf16x8 o2[2][2];
  ob += 2u * 9216u;
  {
    u32x4 wf[2]; wf[0] = wf0;
    wait_raw<0>(r2);
    sfor<8>([&](auto T) {
      constexpr int t = T;
      if constexpr (t == 6) wait_raw<0>(r0);
      sfor<9>([&](auto B) {
        constexpr int b = B;
        if constexpr (b < 8) mfma_acc<32 * b + 4 * t>(asb(wf[t & 1]), of[b]); else mfma_v(a8[t], asb(wf[t & 1]), of[8]);
        GAP();
        if constexpr (b < 4 && t < 7) { perm1<t + 1, b>(wf[(t + 1) & 1], r1); if constexpr (b == 3) PIN(wf[(t + 1) & 1]); }
        if constexpr (t < 2 && b >= 4 && b < 8) { constexpr int j = 4 * t + (b - 4); ld_row_o<(j & 3) * 1024>(r0[j], wo_l, srdO, 15u * 32768u + (j >> 2) * 16384u); }
        if constexpr (t >= 2 && t < 6 && b >= 4 && b < 8) { perm1<2 * (t - 2), b - 4>(F[2 * (t - 2)], r2); perm1<2 * (t - 2) + 1, b - 4>(F[2 * (t - 2) + 1], r2); }
        if constexpr (t >= 6) {
          constexpr int i0 = t == 6 ? (b < 4 ? b : 2 * b - 4) : 14 + 2 * b, n = (t == 6 && b < 4) ? 1 : 2;
          sfor<n>([&](auto K) { constexpr int i = i0 + K; if constexpr (i < 32) perm1<(i >> 2), (i & 3)>(F[8 + (i >> 2)], r0); });
        }
        if constexpr (t == 5 && b == 8) o2[0][0] = *LDS8(ob);
        if constexpr (t == 5 && b == 3) o2[0][1] = *LDS8(ob + 9216u);
        GAP();
      });
    });
    asm volatile("s_nop 1" : "+v"(F[15]));
  }
  const __amdgpu_buffer_rsrc_t srdP = __builtin_amdgcn_make_buffer_rsrc((void*)(part + (size_t)bb * 8u * PART_BYTES), (short)0, 8 * PART_BYTES, 0x00020000);
  {
    const unsigned vo = (2u * wid + (ln >> 5)) * PART_BYTES + hg * 18432u + (ln & 31u) * 16u;
    u32x4 w;
    sfor<9>([&](auto B) {
      constexpr int b = B;
      sfor<16>([&](auto I) {
        constexpr int i = I;
        if constexpr (b < 8) mfma_acc<32 * b + 4 * (i & 7)>(asb(F[i]), o2[b & 1][i >> 3]); else mfma_v(a8[i & 7], asb(F[i]), o2[b & 1][i >> 3]);
        GAP();
        if constexpr (b < 8 && (i == 1 || i == 3)) o2[(b + 1) & 1][i >> 1] = *LDS8(ob + (i >> 1) * 9216u + (b + 1) * 1024u);
        if constexpr (b >= 1 && i >= 2) {
          constexpr int q0 = (i - 2) * 16 / 14, q1 = (i - 1) * 16 / 14;
          sfor<q1 - q0>([&](auto K) {
            constexpr int qq = q0 + K, r = qq >> 2, m = qq & 3;
            const float lo = acc_rd<32 * (b - 1) + 8 * m + r>(), hi = acc_rd<32 * (b - 1) + 8 * m + 4 + r>();
            w[m] = cvtpk(lo, hi);
            if constexpr (m == 3) __builtin_amdgcn_raw_buffer_store_b128(w, srdP, vo + ((b - 1) * 4 + r) * 512u, 0, 16);
          });
        }
        GAP();
      });
    });
    asm volatile("s_nop 7" : "+v"(a8[7]));
    sfor<4>([&](auto R4) {
      constexpr int r = R4;
      const u32x4 w8 = {cvtpk(a8[0][r], a8[1][r]), cvtpk(a8[2][r], a8[3][r]), cvtpk(a8[4][r], a8[5][r]), cvtpk(a8[6][r], a8[7][r])};
      __builtin_amdgcn_raw_buffer_store_b128(w8, srdP, vo + (32 + r) * 512u, 0, 16);
    });
  }
  asm volatile("s_waitcnt vmcnt(0)\n\ts_barrier" ::: "memory");
  gu32 fw2 = (gu32)(cnt + (bb * 8u + (ln & 7u)) * 64u);
  if (wid == 0 && ln == hg) __hip_atomic_store(fw2, f0 + 1u, __ATOMIC_RELAXED, __HIP_MEMORY_SCOPE_AGENT);
  {
    const unsigned t = wid * 64u + ln, t4 = wid < 2u ? t + 1024u : t;
    u32x4 v[8][5];
    unsigned ready = 0;
    sfor<8>([&](auto H) {
      const unsigned src = (hg + H) & 7u;
      if constexpr (H > 0) {
        for (unsigned spin = 0; spin < (1u << 21) && !((ready >> src) & 1u); ++spin) {
          if (spin) __builtin_amdgcn_s_sleep(4);
          ready = (unsigned)__ballot(__hip_atomic_load(fw2, __ATOMIC_RELAXED, __HIP_MEMORY_SCOPE_AGENT) != f0);
        }
        asm volatile("" ::: "memory");
      }
      sfor<4>([&](auto K) { v[H][K] = __builtin_amdgcn_raw_buffer_load_b128(srdP, hg * PART_BYTES + (t + K * 256u) * 16u, src * 18432u, 16); });
      v[H][4] = __builtin_amdgcn_raw_buffer_load_b128(srdP, hg * PART_BYTES + t4 * 16u, src * 18432u, 16);
    });
    sfor<5>([&](auto K) {
      u32x4 o;
      sfor<4>([&](auto E) {
        float lo = 0.f, hi = 0.f;
        sfor<8>([&](auto H) { lo += __uint_as_float(v[H][K][E.value] << 16); hi += __uint_as_float(v[H][K][E.value] & 0xffff0000u); });
        o[E.value] = cvtpk(lo, hi);
      });
      const unsigned item = K < 4 ? t + K * 256u : t4;
      const unsigned unit = item >> 5, row = (unit >> 2) * 16u + (item & 15u), col = hg * 64u + ((item >> 4) & 1u) * 32u + (unit & 3u) * 8u;
      *(u32x4*)(out + ((size_t)(bat * 144u + row) * 1024u + br * 512u + col) * 2u) = o;
    });
  }
}

extern "C" void kernel_launch(void* const* d_in, const int* in_sizes, int n_in, void* d_out, int out_size, void* d_ws, size_t ws_size, hipStream_t stream) {
  (void)hipFuncSetAttribute((const void*)fused_attn, hipFuncAttributeMaxDynamicSharedMemorySize, (int)LDS_BYTES);
  fused_attn<<<256, 256, LDS_BYTES, stream>>>((const char*)d_in[0], (const char*)d_in[1], (const char*)d_in[2], (const char*)d_in[3], (const char*)d_in[4],
                                              (const char*)d_in[5], (const char*)d_in[6], (const char*)d_in[7], (const char*)d_in[8], (const char*)d_in[9], (char*)d_ws + 65536, (unsigned*)d_ws, (char*)d_out);
}
```
